```python
import torch

def _softmax(x, axis=-1):
    return torch.softmax(x, dim=axis)
def _gelu(x):
    return torch.nn.functional.gelu(x, approximate="tanh")
def _silu(x):
    return torch.nn.functional.silu(x)
def _sigmoid(x):
    return torch.sigmoid(x)
def _layernorm(x, axis=-1, eps=1e-5):
    m = x.mean(dim=axis, keepdim=True)
    v = x.var(dim=axis, keepdim=True, unbiased=False)
    return (x - m) / torch.sqrt(v + eps)
def _scatter_add(tgt, idx, src, axis=0):
    return tgt.clone().index_add_(axis, idx, src)
def _segment_sum(src, seg, n):
    out = torch.zeros((n,) + src.shape[1:], dtype=src.dtype)
    return out.index_add_(0, seg, src)
def _pad(x, pw):
    flat = tuple(v for pair in reversed(pw) for v in pair)
    return torch.nn.functional.pad(x, flat)

kernel_name = 'gated_mix__compressed_kv_cla__dense_activation_relu__prompt'

def setup_inputs():

    _w = (lambda a: (lambda t: t.to(torch.bfloat16) if t.is_floating_point() else t)(torch.as_tensor(a)))
    def _A_setup():
        B, H, Sq, Sk, D, M = 1, 32, 2048, 2048, 128, 576
        Dc = 4 * D
        return {
            "x": _w((lambda *s: torch.randn(*s, dtype=torch.bfloat16) * 0.05)(B, Sq, M)),
            "c_kv": _w((lambda *s: torch.randn(*s, dtype=torch.bfloat16) * 0.8)(B, Sk, Dc)),
            "w_q": _w((lambda *s: torch.randn(*s, dtype=torch.bfloat16) * 6.4)(M, H, D) * (M ** -0.5)),
            "w_uk": _w((lambda *s: torch.randn(*s, dtype=torch.bfloat16) * 6.4)(Dc, H, D) * (Dc ** -0.5)),
            "w_uv": _w((lambda *s: torch.randn(*s, dtype=torch.bfloat16) * 0.8)(Dc, H, D) * (Dc ** -0.5)),
            "w_o": _w((lambda *s: torch.randn(*s, dtype=torch.bfloat16) * 0.8)(H, D, M) * ((H * D) ** -0.5)),
        }
    _A_in = _A_setup()

    def _B_setup():
        B, S, D, Dff = 1, 2048, 576, 4608
        return {
            "x": _w((lambda *s: torch.randn(*s, dtype=torch.bfloat16) * 0.05)(B, S, D)),
            "w1": _w((lambda *s: torch.randn(*s, dtype=torch.bfloat16) * 0.05)(D, Dff)),
            "w2": _w((lambda *s: torch.randn(*s, dtype=torch.bfloat16) * 0.05)(Dff, D)),
        }
    _B_in = _B_setup()

    return {
        "A_x": _A_in["x"],
        "A_c_kv": _A_in["c_kv"],
        "A_w_q": _A_in["w_q"],
        "A_w_uk": _A_in["w_uk"],
        "A_w_uv": _A_in["w_uv"],
        "A_w_o": _A_in["w_o"],
        "B_x": _B_in["x"],
        "B_w1": _B_in["w1"],
        "B_w2": _B_in["w2"],
        "gate_w": _w((lambda *s: torch.randn(*s, dtype=torch.bfloat16) * 0.05)(576) * 16.0),
    }

def reference(A_x, A_c_kv, A_w_q, A_w_uk, A_w_uv, A_w_o, B_x, B_w1, B_w2, gate_w):
    def _A_ref(x, c_kv, w_q, w_uk, w_uv, w_o):
        B, Sq, M = x.shape[0], x.shape[1], x.shape[2]
        Sk = c_kv.shape[1]
        H, D = w_q.shape[1], w_q.shape[2]
        q = torch.einsum("bqm,mhd->bhqd", x, w_q)
        k = torch.einsum("bkc,chd->bhkd", c_kv, w_uk)
        v = torch.einsum("bkc,chd->bhkd", c_kv, w_uv)
        scores = torch.einsum("bhqd,bhkd->bhqk", q, k) * (D ** -0.5)
        off = Sk - Sq
        qi = torch.arange(Sq)[:, None]
        kj = torch.arange(Sk)[None, :]
        cmask = kj <= (qi + off)
        scores = torch.where(cmask[None, None], scores, -1e9)
        p = _softmax(scores, axis=-1)
        ctx = torch.einsum("bhqk,bhkd->bhqd", p, v)
        return torch.einsum("bhqd,hdm->bqm", ctx, w_o)

    def _B_ref(x, w1, w2):
        h = torch.einsum("bsd,df->bsf", x, w1)
        zero = torch.zeros_like(h)
        h = torch.maximum(h, zero)
        return torch.einsum("bsf,fd->bsd", h, w2)

    x = A_x
    g = _sigmoid((x * gate_w).sum(-1, keepdims=True))
    a = _A_ref(x, A_c_kv, A_w_q, A_w_uk, A_w_uv, A_w_o)
    b = _B_ref(x, B_w1, B_w2)
    return g * a + (1 - g) * b
```

```cpp
#include <hip/hip_runtime.h>
#include <hip/hip_bf16.h>

using bf16 = __hip_bfloat16;
typedef short bf16x8 __attribute__((ext_vector_type(8)));
typedef short s16x4 __attribute__((ext_vector_type(4)));
typedef float f32x16 __attribute__((ext_vector_type(16)));
typedef unsigned u32x4 __attribute__((ext_vector_type(4)));
typedef unsigned u32x2 __attribute__((ext_vector_type(2)));
typedef __attribute__((address_space(3))) s16x4 lds_s16x4;

constexpr int S = 2048, M = 576, HD = 4096, DC = 512, DFF = 4608;

__device__ __forceinline__ unsigned cvtpk(float lo, float hi) {
  unsigned r; asm("v_cvt_pk_bf16_f32 %0, %1, %2" : "=v"(r) : "v"(lo), "v"(hi)); return r;
}
__device__ __forceinline__ s16x4 trr(const char* p) {
  return __builtin_amdgcn_ds_read_tr16_b64_v4i16((lds_s16x4*)p);
}
__device__ __forceinline__ bf16x8 cat(s16x4 l, s16x4 h) {
  return (bf16x8){l[0], l[1], l[2], l[3], h[0], h[1], h[2], h[3]};
}
__device__ __forceinline__ void glds16(const void* g, void* l) {
  unsigned keep; const unsigned dst = __builtin_amdgcn_readfirstlane((unsigned)(size_t)l);
  asm volatile("s_mov_b32 %0, m0\n\ts_mov_b32 m0, %2\n\ts_nop 0\n\tglobal_load_lds_dwordx4 %1, off\n\ts_mov_b32 m0, %0"
               : "=&s"(keep) : "v"(g), "s"(dst) : "memory");
}
__device__ __forceinline__ int rd_base(int lane) {
  return ((lane & 3) << 3) | (((lane >> 2) & 3) << 6) | (((lane >> 4) & 1) << 5) | (((lane >> 5) & 1) << 8);
}

template <int MB, int NB>
__device__ __forceinline__ void gstage(const bf16* A, int lda, const bf16* B, int ldb, int k0, char* buf, int tid) {
  constexpr int ABYTES = MB * 32 * 128;
#pragma unroll
  for (int i = 0; i < ABYTES / 8192; ++i) {
    int o = i * 8192 + tid * 16, row = o >> 7, c = ((o >> 4) & 7) ^ ((row >> 1) & 7);
    glds16(A + (long)row * lda + k0 + c * 8, buf + o);
  }
  char* bb = buf + ABYTES;
#pragma unroll
  for (int i = 0; i < (NB * 4096 + 8191) / 8192; ++i) {
    int o = i * 8192 + tid * 16;
    if (o >= NB * 4096) o -= 4096;
    {
      int st = o >> 9, kk = (st / NB) * 8 + ((o >> 6) & 7), c = (st % NB) * 32 + ((o >> 4) & 3) * 8;
      int k = (kk & ~0xC) | ((kk & 4) << 1) | ((kk & 8) >> 1);
      glds16(B + (long)(k0 + k) * ldb + c, bb + o);
    }
  }
}

template <int MB, int NB, int NT>
__device__ __forceinline__ void gloop(const bf16* A, int lda, const bf16* B, int ldb, int nk, char* lds,
                                      int mb, int cb0, f32x16* acc) {
  constexpr int ABYTES = MB * 32 * 128, STG = ABYTES + NB * 4096;
  const int tid = threadIdx.x, lane = tid & 63, hi = lane >> 5;
  const int row = mb * 32 + (lane & 31);
  const int aoff = row * 128, asw = (row >> 1) & 7;
  const int boff = ABYTES + rd_base(lane) + cb0 * 512;
  constexpr int NL = ABYTES / 8192 + (NB * 4096 + 8191) / 8192;
  gstage<MB, NB>(A, lda, B, ldb, 0, lds, tid);
  gstage<MB, NB>(A, lda, B, ldb, 64, lds + STG, tid);
  int cs = 0;
  for (int t = 0; t < nk; ++t) {
    if (t + 1 < nk) asm volatile("s_waitcnt vmcnt(%0)" :: "i"(NL) : "memory");
    else asm volatile("s_waitcnt vmcnt(0)" ::: "memory");
    __builtin_amdgcn_s_barrier();
    asm volatile("" ::: "memory");
    char* cur = lds + cs * STG;
    if (t + 2 < nk) gstage<MB, NB>(A, lda, B, ldb, (t + 2) * 64, lds + (cs == 0 ? 2 : cs - 1) * STG, tid);
    cs = cs == 2 ? 0 : cs + 1;
#pragma unroll
    for (int ks = 0; ks < 4; ++ks) {
      bf16x8 a = *(const bf16x8*)(cur + aoff + (((2 * ks + hi) ^ asw) << 4));
#pragma unroll
      for (int j = 0; j < NT; ++j) {
        const char* p = cur + boff + j * 512 + ks * (2 * NB * 512);
        bf16x8 b = cat(trr(p), trr(p + NB * 512));
        acc[j] = __builtin_amdgcn_mfma_f32_32x32x16_bf16(b, a, acc[j], 0, 0, 0);
      }
    }
  }
}

template <int NB, int NT, bool RELU>
__device__ __forceinline__ void proj_body(const bf16* A, int lda, const bf16* B, int ldb, int nk, bf16* C, int ldc,
                                          char* lds, int mb, int cb0) {
  f32x16 acc[NT];
#pragma unroll
  for (int j = 0; j < NT; ++j) acc[j] = f32x16{};
  gloop<4, NB, NT>(A, lda, B, ldb, nk, lds, mb, cb0, acc);
#pragma unroll
  for (int j = 0; j < NT; ++j) asm volatile("s_nop 15\n\ts_nop 15" : "+v"(acc[j]));
  const int lane = threadIdx.x & 63, hi = lane >> 5;
  constexpr int RS = NB * 64 + 16;
  __syncthreads();
  char* cr = lds + (mb * 32 + (lane & 31)) * RS + cb0 * 64 + hi * 8;
#pragma unroll
  for (int j = 0; j < NT; ++j)
#pragma unroll
    for (int g = 0; g < 4; ++g) {
      float v0 = acc[j][4 * g], v1 = acc[j][4 * g + 1], v2 = acc[j][4 * g + 2], v3 = acc[j][4 * g + 3];
      if (RELU) { v0 = fmaxf(v0, 0.f); v1 = fmaxf(v1, 0.f); v2 = fmaxf(v2, 0.f); v3 = fmaxf(v3, 0.f); }
      u32x2 w = {cvtpk(v0, v1), cvtpk(v2, v3)};
      *(u32x2*)(cr + j * 64 + g * 16) = w;
    }
  __syncthreads();
#pragma unroll
  for (int i = threadIdx.x; i < 128 * NB * 4; i += 512) {
    int r = i / (NB * 4), c = i % (NB * 4);
    *(u32x4*)(C + (long)r * ldc + c * 8) = *(const u32x4*)(lds + r * RS + c * 16);
  }
}

__global__ __launch_bounds__(512, 2) void k_proj(const bf16* __restrict__ x, const bf16* __restrict__ ckv,
                                                 const bf16* __restrict__ wq, const bf16* __restrict__ wk,
                                                 const bf16* __restrict__ wv, const bf16* __restrict__ w1,
                                                 bf16* __restrict__ Q, bf16* __restrict__ K, bf16* __restrict__ V,
                                                 bf16* __restrict__ H) {
  extern __shared__ __attribute__((aligned(16))) char lds[];
  const int b = blockIdx.x, type = b >> 8, rt = (b >> 3) & 15, ct = (b & 7) | ((b >> 4) & 8);
  const int w = __builtin_amdgcn_readfirstlane(threadIdx.x >> 6), mb = w & 3, g = w >> 2;
  if (type == 0) {
    const bf16* A = x + (long)rt * 128 * M; const bf16* B = w1 + ct * 288; bf16* C = H + (long)rt * 128 * DFF + ct * 288;
    if (g == 0) proj_body<9, 5, true>(A, M, B, DFF, 9, C, DFF, lds, mb, 0);
    else        proj_body<9, 4, true>(A, M, B, DFF, 9, C, DFF, lds, mb, 5);
  } else {
    const bf16* A = (type == 1 ? x + (long)rt * 128 * M : ckv + (long)rt * 128 * DC);
    const int lda = type == 1 ? M : DC, nk = type == 1 ? 9 : 8;
    const bf16* B = (type == 1 ? wq : type == 2 ? wk : wv) + ct * 256;
    bf16* C = (type == 1 ? Q : type == 2 ? K : V) + (long)rt * 128 * HD + ct * 256;
    proj_body<8, 4, false>(A, lda, B, HD, nk, C, HD, lds, mb, g * 4);
  }
}

constexpr float SC2 = 0.08838834764831845f * 1.4426950408889634f;
__device__ __forceinline__ void kv_stage(const bf16* Kh, const bf16* Vh, int key0, char* kb, char* vb, int tg) {
#pragma unroll
  for (int i = 0; i < 4; ++i) {
    int o = i * 4096 + tg * 16, row = o >> 8, c = ((o >> 4) & 15) ^ (row & 7);
    glds16(Kh + (long)(key0 + row) * HD + c * 8, kb + o);
  }
#pragma unroll
  for (int i = 0; i < 4; ++i) {
    int o = i * 4096 + tg * 16, st = o >> 9, kk = (st >> 2) * 8 + ((o >> 6) & 7), c = (st & 3) * 32 + ((o >> 4) & 3) * 8;
    glds16(Vh + (long)(key0 + kk) * HD + c, vb + o);
  }
}

__global__ __launch_bounds__(512, 2) void k_attn(const bf16* __restrict__ Q, const bf16* __restrict__ K,
                                                 const bf16* __restrict__ V, bf16* __restrict__ ctx) {
  extern __shared__ __attribute__((aligned(16))) char lds[];
  const int tid = threadIdx.x, lane = tid & 63, hi = lane >> 5, r32 = lane & 31;
  const int w = __builtin_amdgcn_readfirstlane(tid >> 6), grp = w >> 2, wq = w & 3, tg = tid & 255;
  const int h = blockIdx.x & 31, pr = blockIdx.x >> 5;
  const bf16* Kh = K + h * 128; const bf16* Vh = V + h * 128;
  char* gl = lds + grp * 65536;
  const int vrd = rd_base(lane);
  for (int pass = 0; pass < 2; ++pass) {
    const int qb = pass == 0 ? 15 - pr : pr;
    const int qrow = qb * 128 + wq * 32 + r32;
    bf16x8 qr[8];
#pragma unroll
    for (int d = 0; d < 8; ++d) qr[d] = *(const bf16x8*)(Q + (long)qrow * HD + h * 128 + d * 16 + hi * 8);
    f32x16 o[4] = {};
    float m_ = -1e30f, l_ = 0.f;
    const int nt = qb + 1;
    kv_stage(Kh, Vh, grp * 64, gl, gl + 16384, tg);
    asm volatile("s_waitcnt vmcnt(0)" ::: "memory"); __syncthreads();
    for (int t = 0; t < nt; ++t) {
      char* kb = gl + (t & 1) * 32768; char* vb = kb + 16384;
      if (t + 1 < nt) { char* nb = gl + ((t + 1) & 1) * 32768; kv_stage(Kh, Vh, (2 * (t + 1) + grp) * 64, nb, nb + 16384, tg); }
      f32x16 p0 = {}, p1 = {};
#pragma unroll
      for (int d = 0; d < 8; ++d) {
        int cb = (d * 2 + hi) << 4;
        bf16x8 k0 = *(const bf16x8*)(kb + r32 * 256 + (cb ^ ((r32 & 7) << 4)));
        bf16x8 k1 = *(const bf16x8*)(kb + (32 + r32) * 256 + (cb ^ ((r32 & 7) << 4)));
        p0 = __builtin_amdgcn_mfma_f32_32x32x16_bf16(k0, qr[d], p0, 0, 0, 0);
        p1 = __builtin_amdgcn_mfma_f32_32x32x16_bf16(k1, qr[d], p1, 0, 0, 0);
      }
      if (t == nt - 1) {
        int dq = qrow - (2 * t + grp) * 64 - 4 * hi;
#pragma unroll
        for (int r = 0; r < 16; ++r) {
          int c = (r & 3) + 8 * (r >> 2);
          if (c > dq) p0[r] = -__builtin_inff();
          if (c + 32 > dq) p1[r] = -__builtin_inff();
        }
      }
      float mx = p0[0];
#pragma unroll
      for (int r = 1; r < 16; ++r) mx = fmaxf(mx, p0[r]);
#pragma unroll
      for (int r = 0; r < 16; ++r) mx = fmaxf(mx, p1[r]);
      { auto rr = __builtin_amdgcn_permlane32_swap(__float_as_uint(mx), __float_as_uint(mx), false, false);
        mx = fmaxf(__uint_as_float(rr[0]), __uint_as_float(rr[1])); }
      float mn = fmaxf(m_, mx), al = __builtin_amdgcn_exp2f((m_ - mn) * SC2), mc = -mn * SC2, ps = 0.f;
      m_ = mn;
#pragma unroll
      for (int r = 0; r < 16; ++r) { p0[r] = __builtin_amdgcn_exp2f(fmaf(p0[r], SC2, mc)); ps += p0[r]; }
#pragma unroll
      for (int r = 0; r < 16; ++r) { p1[r] = __builtin_amdgcn_exp2f(fmaf(p1[r], SC2, mc)); ps += p1[r]; }
      l_ = l_ * al + ps;
#pragma unroll
      for (int d = 0; d < 4; ++d) o[d] *= al;
      bf16x8 pf[4];
#pragma unroll
      for (int s = 0; s < 2; ++s) {
        u32x4 a = {cvtpk(p0[8 * s], p0[8 * s + 1]), cvtpk(p0[8 * s + 2], p0[8 * s + 3]), cvtpk(p0[8 * s + 4], p0[8 * s + 5]), cvtpk(p0[8 * s + 6], p0[8 * s + 7])};
        u32x4 b = {cvtpk(p1[8 * s], p1[8 * s + 1]), cvtpk(p1[8 * s + 2], p1[8 * s + 3]), cvtpk(p1[8 * s + 4], p1[8 * s + 5]), cvtpk(p1[8 * s + 6], p1[8 * s + 7])};
        pf[s] = *(bf16x8*)&a; pf[2 + s] = *(bf16x8*)&b;
      }
#pragma unroll
      for (int d = 0; d < 4; ++d)
#pragma unroll
        for (int ks = 0; ks < 4; ++ks) {
          const char* p = vb + vrd + d * 512 + ks * 4096;
          bf16x8 vf = cat(trr(p), trr(p + 2048));
          o[d] = __builtin_amdgcn_mfma_f32_32x32x16_bf16(vf, pf[ks], o[d], 0, 0, 0);
        }
      asm volatile("s_waitcnt vmcnt(0)" ::: "memory"); __syncthreads();
    }
    { auto rr = __builtin_amdgcn_permlane32_swap(__float_as_uint(l_), __float_as_uint(l_), false, false);
      l_ = __uint_as_float(rr[0]) + __uint_as_float(rr[1]); }
    float* ex = (float*)lds + wq * (66 * 64);
    if (grp == 1) {
#pragma unroll
      for (int d = 0; d < 4; ++d)
#pragma unroll
        for (int r = 0; r < 16; ++r) ex[(d * 16 + r) * 64 + lane] = o[d][r];
      ex[64 * 64 + lane] = m_; ex[65 * 64 + lane] = l_;
    }
    asm volatile("s_waitcnt vmcnt(0)" ::: "memory"); __syncthreads();
    if (grp == 0) {
      float m1 = ex[64 * 64 + lane], l1 = ex[65 * 64 + lane], mn = fmaxf(m_, m1);
      float a0 = __builtin_amdgcn_exp2f((m_ - mn) * SC2), a1 = __builtin_amdgcn_exp2f((m1 - mn) * SC2);
      float inv = 1.f / (l_ * a0 + l1 * a1); a0 *= inv; a1 *= inv;
      bf16* cr = ctx + (long)qrow * HD + h * 128 + hi * 4;
#pragma unroll
      for (int d = 0; d < 4; ++d)
#pragma unroll
        for (int g = 0; g < 4; ++g) {
          float v[4];
#pragma unroll
          for (int e = 0; e < 4; ++e) v[e] = o[d][4 * g + e] * a0 + ex[(d * 16 + 4 * g + e) * 64 + lane] * a1;
          u32x2 ww = {cvtpk(v[0], v[1]), cvtpk(v[2], v[3])};
          *(u32x2*)(cr + d * 32 + g * 8) = ww;
        }
    }
    asm volatile("s_waitcnt vmcnt(0)" ::: "memory"); __syncthreads();
  }
}

template <int NT>
__device__ __forceinline__ void out_body(const bf16* A, int lda, const bf16* B, int nk, float* C, char* lds, int mb, int cb0) {
  f32x16 acc[NT];
#pragma unroll
  for (int j = 0; j < NT; ++j) acc[j] = f32x16{};
  gloop<2, 9, NT>(A, lda, B, M, nk, lds, mb, cb0, acc);
  const int lane = threadIdx.x & 63, hi = lane >> 5;
  float* cr = C + (long)(mb * 32 + (lane & 31)) * M + cb0 * 32 + hi * 4;
#pragma unroll
  for (int j = 0; j < NT; ++j)
#pragma unroll
    for (int g = 0; g < 4; ++g) {
      float4 v = {acc[j][4 * g], acc[j][4 * g + 1], acc[j][4 * g + 2], acc[j][4 * g + 3]};
      *(float4*)(cr + j * 32 + g * 8) = v;
    }
}
__global__ __launch_bounds__(512, 2) void k_out(const bf16* __restrict__ ctx, const bf16* __restrict__ H,
                                                const bf16* __restrict__ wo, const bf16* __restrict__ w2,
                                                float* __restrict__ slab) {
  extern __shared__ __attribute__((aligned(16))) char lds[];
  const int b = blockIdx.x, rt = b & 31, ct = (b >> 5) & 1, sp = b >> 6;
  const int w = __builtin_amdgcn_readfirstlane(threadIdx.x >> 6), mb = w & 1, g = w >> 1;
  const bf16 *A, *B; int lda, nk;
  if (sp < 2) { lda = HD; nk = 32; A = ctx + (long)rt * 64 * HD + sp * 2048; B = wo + (long)sp * 2048 * M + ct * 288; }
  else { lda = DFF; nk = 36; A = H + (long)rt * 64 * DFF + (sp - 2) * 2304; B = w2 + (long)(sp - 2) * 2304 * M + ct * 288; }
  float* C = slab + (long)sp * S * M + (long)rt * 64 * M + ct * 288;
  if (g == 0) out_body<3>(A, lda, B, nk, C, lds, mb, 0);
  else out_body<2>(A, lda, B, nk, C, lds, mb, 1 + 2 * g);
}

__device__ __forceinline__ float rbf(float v) { return __bfloat162float(__float2bfloat16(v)); }
__global__ __launch_bounds__(256) void k_fin(const bf16* __restrict__ x, const bf16* __restrict__ gw,
                                             const float* __restrict__ slab, bf16* __restrict__ out) {
  const int row = blockIdx.x * 4 + (threadIdx.x >> 6), lane = threadIdx.x & 63;
  float s = 0.f;
  for (int c = lane; c < M; c += 64) s += rbf(__bfloat162float(x[row * M + c]) * __bfloat162float(gw[c]));
#pragma unroll
  for (int o = 32; o; o >>= 1) s += __shfl_xor(s, o);
  s = rbf(s);
  float g = rbf(1.f / (1.f + __expf(-s))), g1 = rbf(1.f - g);
  for (int c = lane; c < M; c += 64) {
    long i = (long)row * M + c;
    float a = rbf(slab[i] + slab[i + (long)S * M]), b = rbf(slab[i + 2L * S * M] + slab[i + 3L * S * M]);
    out[i] = __float2bfloat16(rbf(g * a) + rbf(g1 * b));
  }
}

extern "C" void kernel_launch(void* const* d_in, const int* in_sizes, int n_in, void* d_out, int out_size,
                              void* d_ws, size_t ws_size, hipStream_t stream) {
  const bf16 *x = (const bf16*)d_in[0], *ckv = (const bf16*)d_in[1], *wq = (const bf16*)d_in[2], *wk = (const bf16*)d_in[3],
             *wv = (const bf16*)d_in[4], *wo = (const bf16*)d_in[5], *w1 = (const bf16*)d_in[7], *w2 = (const bf16*)d_in[8],
             *gw = (const bf16*)d_in[9];
  char* ws = (char*)d_ws;
  const size_t QB = (size_t)S * HD * 2;
  bf16 *Q = (bf16*)ws, *K = (bf16*)(ws + QB), *V = (bf16*)(ws + 2 * QB), *ctx = (bf16*)(ws + 3 * QB), *H = (bf16*)(ws + 4 * QB);
  float* slab = (float*)(ws + 4 * QB + (size_t)S * DFF * 2);
  const int l1 = 3 * (16384 + 9 * 4096), l2 = 131072, l3 = 3 * (8192 + 9 * 4096);
  hipFuncSetAttribute((const void*)k_proj, hipFuncAttributeMaxDynamicSharedMemorySize, l1);
  hipFuncSetAttribute((const void*)k_attn, hipFuncAttributeMaxDynamicSharedMemorySize, l2);
  hipFuncSetAttribute((const void*)k_out, hipFuncAttributeMaxDynamicSharedMemorySize, l3);
  k_proj<<<1024, 512, l1, stream>>>(x, ckv, wq, wk, wv, w1, Q, K, V, H);
  k_attn<<<256, 512, l2, stream>>>(Q, K, V, ctx);
  k_out<<<256, 512, l3, stream>>>(ctx, H, wo, w2, slab);
  k_fin<<<512, 256, 0, stream>>>(x, gw, slab, (bf16*)d_out);
}
```
